```python
import torch

def _softmax(x, axis=-1):
    return torch.softmax(x, dim=axis)
def _gelu(x):
    return torch.nn.functional.gelu(x, approximate="tanh")
def _silu(x):
    return torch.nn.functional.silu(x)
def _sigmoid(x):
    return torch.sigmoid(x)
def _layernorm(x, axis=-1, eps=1e-5):
    m = x.mean(dim=axis, keepdim=True)
    v = x.var(dim=axis, keepdim=True, unbiased=False)
    return (x - m) / torch.sqrt(v + eps)
def _scatter_add(tgt, idx, src, axis=0):
    return tgt.clone().index_add_(axis, idx, src)
def _segment_sum(src, seg, n):
    out = torch.zeros((n,) + src.shape[1:], dtype=src.dtype)
    return out.index_add_(0, seg, src)
def _pad(x, pw):
    flat = tuple(v for pair in reversed(pw) for v in pair)
    return torch.nn.functional.pad(x, flat)

kernel_name = 'sequential_preLN__gqa_block_cached__geglu_ffn__decode'

def setup_inputs():

    _w = (lambda a: (lambda t: t.to(torch.bfloat16) if t.is_floating_point() else t)(torch.as_tensor(a)))
    def _A_setup():
        B, Sq, D = 4, 1, 512
        Hq, Hkv, Sk, Dh = 32, 8, 16384, 128
        assert Hq % Hkv == 0, (Hq, Hkv)
        return {
            "x": _w((lambda *s: torch.randn(*s, dtype=torch.bfloat16) * 0.05)(B, Sq, D)),
            "k_cache": _w((lambda *s: torch.randn(*s, dtype=torch.bfloat16) * 3.2)(B, Hkv, Sk, Dh)),
            "v_cache": _w((lambda *s: torch.randn(*s, dtype=torch.bfloat16) * 0.4)(B, Hkv, Sk, Dh)),
            "wq": _w((lambda *s: torch.randn(*s, dtype=torch.bfloat16) * 3.2)(D, Hq * Dh) * (D ** -0.5)),
            "wo": _w((lambda *s: torch.randn(*s, dtype=torch.bfloat16) * 0.4)(Hq * Dh, D) * ((Hq * Dh) ** -0.5)),
        }
    _A_in = _A_setup()

    def _B_setup():
        B, S, D, F = 4, 1, 512, 1792
        return {
            "x": _w((lambda *s: torch.randn(*s, dtype=torch.bfloat16) * 0.05)(B, S, D)),
            "w_gate": _w((lambda *s: torch.randn(*s, dtype=torch.bfloat16) * 0.05)(D, F)),
            "w_up": _w((lambda *s: torch.randn(*s, dtype=torch.bfloat16) * 0.05)(D, F)),
            "w_down": _w((lambda *s: torch.randn(*s, dtype=torch.bfloat16) * 0.05)(F, D)),
        }
    _B_in = _B_setup()

    return {
        "A_x": _A_in["x"],
        "A_k_cache": _A_in["k_cache"],
        "A_v_cache": _A_in["v_cache"],
        "A_wq": _A_in["wq"],
        "A_wo": _A_in["wo"],
        "B_x": _B_in["x"],
        "B_w_gate": _B_in["w_gate"],
        "B_w_up": _B_in["w_up"],
        "B_w_down": _B_in["w_down"],
    }

def reference(A_x, A_k_cache, A_v_cache, A_wq, A_wo, B_x, B_w_gate, B_w_up, B_w_down):
    def _A_ref(x, k_cache, v_cache, wq, wo):
        B, Sq, D = x.shape
        Hq, Dh = 32, 128
        Hkv = k_cache.shape[1]
        rep = Hq // Hkv
        q = (lambda x, *d: x.permute(*d))((x @ wq).reshape(B, Sq, Hq, Dh), 0, 2, 1, 3)
        k = (lambda x, n, axis: torch.repeat_interleave(x, n, dim=axis))(k_cache, rep, axis=1)
        v = (lambda x, n, axis: torch.repeat_interleave(x, n, dim=axis))(v_cache, rep, axis=1)
        scores = torch.einsum("bhqd,bhkd->bhqk", q, k) * (Dh ** -0.5)
        p = _softmax(scores, axis=-1)
        o = torch.einsum("bhqk,bhkd->bhqd", p, v)
        return (lambda x, *d: x.permute(*d))(o, 0, 2, 1, 3).reshape(B, Sq, Hq * Dh) @ wo

    def _B_ref(x, w_gate, w_up, w_down):
        return (_gelu(x @ w_gate) * (x @ w_up)) @ w_down

    x = A_x
    h = x + _A_ref(_layernorm(x, axis=-1), A_k_cache, A_v_cache, A_wq, A_wo)
    h2 = h + _B_ref(_layernorm(h, axis=-1), B_w_gate, B_w_up, B_w_down)
    return h2
```

```cpp
#include <hip/hip_runtime.h>
#include <hip/hip_bf16.h>
#include <cstdint>

#define EMU 0

typedef unsigned v4u __attribute__((ext_vector_type(4)));
typedef unsigned v2u __attribute__((ext_vector_type(2)));
typedef float f32x4 __attribute__((ext_vector_type(4)));
typedef short bf16x8 __attribute__((ext_vector_type(8)));
typedef short bf16x4 __attribute__((ext_vector_type(4)));
typedef unsigned short u16;

constexpr int B = 4, D = 512, HQ = 32, HKV = 8, SK = 16384, DH = 128, F = 1792;
constexpr int NSPLIT = 8;

__device__ __forceinline__ float bf2f(u16 b) { return __builtin_bit_cast(float, (unsigned)b << 16); }
typedef float f32x2 __attribute__((ext_vector_type(2)));
typedef __bf16 bf16x2_t __attribute__((ext_vector_type(2)));
__device__ __forceinline__ unsigned cvtpk(float lo, float hi) {
  f32x2 v = {lo, hi};
  return __builtin_bit_cast(unsigned, __builtin_convertvector(v, bf16x2_t));
}
__device__ __forceinline__ float rbf(float x) { return __builtin_bit_cast(float, cvtpk(x, x) << 16); }
__device__ __forceinline__ float rb(float x) { return EMU ? rbf(x) : x; }
template <int CTRL> __device__ __forceinline__ float dpp(float x) {
  return __builtin_bit_cast(float, __builtin_amdgcn_mov_dpp(__builtin_bit_cast(int, x), CTRL, 0xf, 0xf, true));
}
__device__ __forceinline__ float wave_sum(float x) {
  for (int o = 32; o > 0; o >>= 1) x += __shfl_xor(x, o);
  return x;
}

__device__ __forceinline__ void ln_row(float (&v)[8]) {
  float s = 0.f;
  for (int i = 0; i < 8; ++i) s += v[i];
  s = wave_sum(s);
  float mean = s * (1.f / D);
  float q = 0.f;
  for (int i = 0; i < 8; ++i) { float d = v[i] - mean; q += d * d; }
  q = wave_sum(q);
  float var = q * (1.f / D);
  float mr = rb(mean), vr = rb(var);
  float den = rb(sqrtf(rb(vr + 1e-5f)));
  for (int i = 0; i < 8; ++i) v[i] = rbf(rb(v[i] - mr) / den);
}

__global__ void __launch_bounds__(256) k_q(const u16* x, const u16* wq, u16* qout) {
  __shared__ float xn[B][D];
  __shared__ float red[8][B][64];
  const int tid = threadIdx.x, lane = tid & 63, w = tid >> 6;
  {
    float v[8];
    v4u xv = *(const v4u*)(x + w * D + lane * 8);
    for (int i = 0; i < 4; ++i) { v[2 * i] = __builtin_bit_cast(float, xv[i] << 16); v[2 * i + 1] = __builtin_bit_cast(float, xv[i] & 0xffff0000u); }
    ln_row(v);
    for (int i = 0; i < 8; ++i) xn[w][lane * 8 + i] = v[i];
  }
  __syncthreads();
  const int cg = tid & 7, rg = tid >> 3;
  const int c0 = blockIdx.x * 64 + cg * 8;
  float acc[B][8] = {};
  for (int k = rg; k < D; k += 32) {
    v4u wv = *(const v4u*)(wq + (size_t)k * (HQ * DH) + c0);
    float wf[8];
    for (int i = 0; i < 4; ++i) { wf[2 * i] = __builtin_bit_cast(float, wv[i] << 16); wf[2 * i + 1] = __builtin_bit_cast(float, wv[i] & 0xffff0000u); }
    for (int b = 0; b < B; ++b) { float xv = xn[b][k]; for (int i = 0; i < 8; ++i) acc[b][i] = fmaf(xv, wf[i], acc[b][i]); }
  }
  for (int b = 0; b < B; ++b) for (int i = 0; i < 8; ++i) { float t = acc[b][i]; t += __shfl_xor(t, 8); t += __shfl_xor(t, 16); t += __shfl_xor(t, 32); acc[b][i] = t; }
  if (lane < 8) for (int b = 0; b < B; ++b) for (int i = 0; i < 8; ++i) red[w][b][lane * 8 + i] = acc[b][i];
  __syncthreads();
  { int b = tid >> 6, c = tid & 63; float t = red[0][b][c] + red[1][b][c] + red[2][b][c] + red[3][b][c];
    qout[b * (HQ * DH) + blockIdx.x * 64 + c] = (u16)(cvtpk(t, t) & 0xffff); }
}

constexpr int NW = 8, KEYS_WG = SK / NSPLIT, KEYS_W = KEYS_WG / NW;
constexpr int BLK = 8;
constexpr int NBLK = KEYS_W / (4 * BLK);
constexpr int WIN = 32;
constexpr int NLD = NBLK * BLK * 2;
__device__ __forceinline__ v4u ldnt(const v4u* p) { return __builtin_nontemporal_load(p); }

__global__ void __launch_bounds__(512, 2) k_attn(const u16* q, const u16* kc, const u16* vc, float* part) {
  __shared__ float mrg[NW][4][DH + 4];
  const int tid = threadIdx.x, lane = tid & 63, w = __builtin_amdgcn_readfirstlane(tid >> 6);
  const int bh = blockIdx.x >> 3, sp = blockIdx.x & 7;
  const int b = bh >> 3, kvh = bh & 7;
  const int g = lane >> 4, r = (lane >> 2) & 3, c = lane & 3;
  const size_t base = ((size_t)bh * SK + (size_t)sp * KEYS_WG) * DH;
  const v4u* kp = (const v4u*)(kc + base) + (size_t)w * (32 * 16) + r * 16 + g * 4 + c;
  const v4u* vp = (const v4u*)(vc + base) + (size_t)w * (32 * 16) + lane;
  auto addr = [&](int i) -> const v4u* {
    const int blk = i / (2 * BLK), t = i % (2 * BLK);
    return (t < BLK ? kp + t * 64 : vp + (t - BLK) * 64) + (size_t)blk * (NW * 32 * 16);
  };
  v4u a[WIN];
#pragma unroll
  for (int i = 0; i < WIN; ++i) a[i] = ldnt(addr(i));
  __builtin_amdgcn_sched_barrier(0);
  const int hq = (lane >> 2) & 3;
  v4u qf = *(const v4u*)(q + (size_t)b * (HQ * DH) + (kvh * 4 + hq) * DH + g * 32 + c * 8);
  const unsigned mask0 = c == 0 ? 0x0000ffffu : (c == 1 ? 0xffff0000u : 0u);
  const unsigned mask1 = c == 2 ? 0x0000ffffu : (c == 3 ? 0xffff0000u : 0u);
  constexpr float SCALE = 0.08838834764831845f, L2E = 1.4426950408889634f;
  float m = -INFINITY, l = 0.f;
  f32x4 acc0 = {0, 0, 0, 0}, acc1 = {0, 0, 0, 0};
#pragma unroll
  for (int blk = 0; blk < NBLK; ++blk) {
    float s[BLK];
#pragma unroll
    for (int t = 0; t < BLK; ++t) {
      const int i = blk * 2 * BLK + t;
      const v4u cur = a[i % WIN];
      if (i + WIN < NLD) a[i % WIN] = ldnt(addr(i + WIN));
      f32x4 d = __builtin_amdgcn_mfma_f32_16x16x32_bf16(__builtin_bit_cast(bf16x8, cur), __builtin_bit_cast(bf16x8, qf), f32x4{0, 0, 0, 0}, 0, 0, 0);
      float x = dpp<0x00>(d[0]) + dpp<0x55>(d[1]);
      x += dpp<0xAA>(d[2]);
      x += dpp<0xFF>(d[3]);
      if (EMU) x = rbf(rbf(x) * SCALE); else x = x * SCALE;
      s[t] = x;
      __builtin_amdgcn_sched_barrier(0);
    }
    float bm = s[0];
#pragma unroll
    for (int t = 1; t < BLK; ++t) bm = fmaxf(bm, s[t]);
    bm = fmaxf(bm, __shfl_xor(bm, 16));
    bm = fmaxf(bm, __shfl_xor(bm, 32));
    const float mn = fmaxf(m, bm);
    const float alpha = __builtin_amdgcn_exp2f((m - mn) * L2E);
    m = mn;
    l *= alpha;
    const float al = __shfl(alpha, (lane >> 4) * 4);
    acc0 *= al; acc1 *= al;
    const float mb = -mn * L2E;
#pragma unroll
    for (int t = 0; t < BLK; ++t) {
      const int i = blk * 2 * BLK + BLK + t;
      const v4u cur = a[i % WIN];
      if (i + WIN < NLD) a[i % WIN] = ldnt(addr(i + WIN));
      const float p = __builtin_amdgcn_exp2f(fmaf(s[t], L2E, mb));
      l += p;
      const unsigned pk = cvtpk(p, p);
      v2u pa = {pk & mask0, pk & mask1};
      v2u b0 = {cur[0], cur[1]}, b1 = {cur[2], cur[3]};
      acc0 = __builtin_amdgcn_mfma_f32_16x16x16bf16_1k(__builtin_bit_cast(bf16x4, pa), __builtin_bit_cast(bf16x4, b0), acc0, 0, 0, 0);
      acc1 = __builtin_amdgcn_mfma_f32_16x16x16bf16_1k(__builtin_bit_cast(bf16x4, pa), __builtin_bit_cast(bf16x4, b1), acc1, 0, 0, 0);
      __builtin_amdgcn_sched_barrier(0);
    }
  }
  l += __shfl_xor(l, 16); l += __shfl_xor(l, 32);
  {
    float* rec = mrg[w][lane >> 4];
    *(f32x4*)(rec + 4 + (lane & 15) * 8) = acc0;
    *(f32x4*)(rec + 4 + (lane & 15) * 8 + 4) = acc1;
    if (lane < 16 && c == 0) { mrg[w][hq][0] = m; mrg[w][hq][1] = l; }
  }
  __syncthreads();
  {
    const int h = tid >> 7, d = tid & 127;
    float M = -INFINITY;
    for (int ww = 0; ww < NW; ++ww) M = fmaxf(M, mrg[ww][h][0]);
    float L = 0.f, o = 0.f;
    for (int ww = 0; ww < NW; ++ww) { float wt = __builtin_amdgcn_exp2f((mrg[ww][h][0] - M) * L2E); L = fmaf(wt, mrg[ww][h][1], L); o = fmaf(wt, mrg[ww][h][4 + d], o); }
    float* rec = part + ((size_t)(bh * 4 + h) * NSPLIT + sp) * 132;
    rec[4 + d] = o;
    if (d == 0) { rec[0] = M; rec[1] = L; }
  }
}

__global__ void __launch_bounds__(256) k_wo(const float* part, const u16* wo, float* hp) {
  __shared__ float o[B][64];
  __shared__ float red[4][B][D];
  const int tid = threadIdx.x;
  const int k0 = blockIdx.x * 64, h = k0 / DH, d0 = k0 % DH;
  {
    const int b = tid >> 6, d = d0 + (tid & 63);
    const float* rec = part + (size_t)(b * HQ + h) * NSPLIT * 132;
    float M = -INFINITY;
    for (int s = 0; s < NSPLIT; ++s) M = fmaxf(M, rec[s * 132]);
    float L = 0.f, acc = 0.f;
    for (int s = 0; s < NSPLIT; ++s) { float wt = __expf(rec[s * 132] - M); L = fmaf(wt, rec[s * 132 + 1], L); acc = fmaf(wt, rec[s * 132 + 4 + d], acc); }
    o[b][tid & 63] = rb(acc / L);
  }
  __syncthreads();
  const int lane = tid & 63, w = tid >> 6;
  float acc[B][8] = {};
  for (int k = w; k < 64; k += 4) {
    v4u wv = *(const v4u*)(wo + (size_t)(k0 + k) * D + lane * 8);
    float wf[8];
    for (int i = 0; i < 4; ++i) { wf[2 * i] = __builtin_bit_cast(float, wv[i] << 16); wf[2 * i + 1] = __builtin_bit_cast(float, wv[i] & 0xffff0000u); }
    for (int b = 0; b < B; ++b) { float xv = o[b][k]; for (int i = 0; i < 8; ++i) acc[b][i] = fmaf(xv, wf[i], acc[b][i]); }
  }
  for (int b = 0; b < B; ++b) for (int i = 0; i < 8; ++i) red[w][b][lane * 8 + i] = acc[b][i];
  __syncthreads();
  for (int i = tid; i < B * D; i += 256) { int b = i / D, c = i % D; hp[(size_t)blockIdx.x * (B * D) + i] = red[0][b][c] + red[1][b][c] + red[2][b][c] + red[3][b][c]; }
}

__device__ __forceinline__ float gelu_tanh(float x) {
  float u = 0.7978845608028654f * (x + 0.044715f * x * x * x);
  return 0.5f * x * (1.f + tanhf(u));
}

__global__ void __launch_bounds__(256) k_ffn(const float* hp, const u16* x, const u16* wg, const u16* wu, const u16* wd, float* hout, float* dp) {
  __shared__ float hn[B][D];
  __shared__ float redg[4][B][32], redu[4][B][32];
  __shared__ float act[B][32];
  __shared__ float red[4][B][D];
  const int tid = threadIdx.x, lane = tid & 63, w = tid >> 6;
  {
    float v[8] = {};
    for (int p = 0; p < 64; ++p) {
      const f32x4* src = (const f32x4*)(hp + (size_t)p * (B * D) + w * D + lane * 8);
      f32x4 a0 = src[0], a1 = src[1];
      for (int i = 0; i < 4; ++i) { v[i] += a0[i]; v[4 + i] += a1[i]; }
    }
    for (int i = 0; i < 8; ++i) v[i] = rb(bf2f(x[w * D + lane * 8 + i]) + rb(v[i]));
    if (blockIdx.x == 0) for (int i = 0; i < 8; ++i) hout[w * D + lane * 8 + i] = v[i];
    ln_row(v);
    for (int i = 0; i < 8; ++i) hn[w][lane * 8 + i] = v[i];
  }
  __syncthreads();
  const int f0 = blockIdx.x * 32;
  {
    const int cg = tid & 3, rg = tid >> 2;
    float ag[B][8] = {}, au[B][8] = {};
    for (int k = rg; k < D; k += 64) {
      v4u gv = *(const v4u*)(wg + (size_t)k * F + f0 + cg * 8);
      v4u uv = *(const v4u*)(wu + (size_t)k * F + f0 + cg * 8);
      for (int i = 0; i < 4; ++i) {
        float g0 = __builtin_bit_cast(float, gv[i] << 16), g1 = __builtin_bit_cast(float, gv[i] & 0xffff0000u);
        float u0 = __builtin_bit_cast(float, uv[i] << 16), u1 = __builtin_bit_cast(float, uv[i] & 0xffff0000u);
        for (int b = 0; b < B; ++b) { float xv = hn[b][k];
          ag[b][2 * i] = fmaf(xv, g0, ag[b][2 * i]); ag[b][2 * i + 1] = fmaf(xv, g1, ag[b][2 * i + 1]);
          au[b][2 * i] = fmaf(xv, u0, au[b][2 * i]); au[b][2 * i + 1] = fmaf(xv, u1, au[b][2 * i + 1]); }
      }
    }
    for (int b = 0; b < B; ++b) for (int i = 0; i < 8; ++i) {
      float t = ag[b][i]; t += __shfl_xor(t, 4); t += __shfl_xor(t, 8); t += __shfl_xor(t, 16); t += __shfl_xor(t, 32); ag[b][i] = t;
      t = au[b][i]; t += __shfl_xor(t, 4); t += __shfl_xor(t, 8); t += __shfl_xor(t, 16); t += __shfl_xor(t, 32); au[b][i] = t; }
    if (lane < 4) for (int b = 0; b < B; ++b) for (int i = 0; i < 8; ++i) { redg[w][b][lane * 8 + i] = ag[b][i]; redu[w][b][lane * 8 + i] = au[b][i]; }
  }
  __syncthreads();
  if (tid < 128) { int b = tid >> 5, c = tid & 31;
    float gg = rb(redg[0][b][c] + redg[1][b][c] + redg[2][b][c] + redg[3][b][c]);
    float uu = rb(redu[0][b][c] + redu[1][b][c] + redu[2][b][c] + redu[3][b][c]);
    act[b][c] = rb(rb(gelu_tanh(gg)) * uu); }
  __syncthreads();
  float acc[B][8] = {};
  for (int k = w; k < 32; k += 4) {
    v4u wv = *(const v4u*)(wd + (size_t)(f0 + k) * D + lane * 8);
    float wf[8];
    for (int i = 0; i < 4; ++i) { wf[2 * i] = __builtin_bit_cast(float, wv[i] << 16); wf[2 * i + 1] = __builtin_bit_cast(float, wv[i] & 0xffff0000u); }
    for (int b = 0; b < B; ++b) { float xv = act[b][k]; for (int i = 0; i < 8; ++i) acc[b][i] = fmaf(xv, wf[i], acc[b][i]); }
  }
  for (int b = 0; b < B; ++b) for (int i = 0; i < 8; ++i) red[w][b][lane * 8 + i] = acc[b][i];
  __syncthreads();
  for (int i = tid; i < B * D; i += 256) { int b = i / D, c = i % D; dp[(size_t)blockIdx.x * (B * D) + i] = red[0][b][c] + red[1][b][c] + red[2][b][c] + red[3][b][c]; }
}

__global__ void __launch_bounds__(256) k_fin(const float* dp, const float* h, u16* out) {
  const int i = blockIdx.x * 256 + threadIdx.x;
  float s = 0.f;
  for (int p = 0; p < 56; ++p) s += dp[(size_t)p * (B * D) + i];
  float r = h[i] + rb(s);
  out[i] = (u16)(cvtpk(r, r) & 0xffff);
}

extern "C" void kernel_launch(void* const* d_in, const int* in_sizes, int n_in, void* d_out, int out_size, void* d_ws, size_t ws_size, hipStream_t stream) {
  const u16* x = (const u16*)d_in[0];
  const u16* kc = (const u16*)d_in[1];
  const u16* vc = (const u16*)d_in[2];
  const u16* wq = (const u16*)d_in[3];
  const u16* wo = (const u16*)d_in[4];
  const u16* wg = (const u16*)d_in[6];
  const u16* wu = (const u16*)d_in[7];
  const u16* wd = (const u16*)d_in[8];
  char* ws = (char*)d_ws;
  u16* q = (u16*)ws;
  float* part = (float*)(ws + (1 << 16));
  float* hp = (float*)(ws + (1 << 20));
  float* dp = (float*)(ws + (2 << 20));
  float* h = (float*)(ws + (3 << 20));
  k_q<<<64, 256, 0, stream>>>(x, wq, q);
  k_attn<<<256, 512, 0, stream>>>(q, kc, vc, part);
  k_wo<<<64, 256, 0, stream>>>(part, wo, hp);
  k_ffn<<<56, 256, 0, stream>>>(hp, x, wg, wu, wd, h, dp);
  k_fin<<<8, 256, 0, stream>>>(dp, h, (u16*)d_out);
}
```

```cpp
#include <hip/hip_runtime.h>
#include <cstdint>

typedef unsigned v4u __attribute__((ext_vector_type(4)));
typedef unsigned v2u __attribute__((ext_vector_type(2)));
typedef float f32x4 __attribute__((ext_vector_type(4)));
typedef float f32x2 __attribute__((ext_vector_type(2)));
typedef short s16x8 __attribute__((ext_vector_type(8)));
typedef short s16x4 __attribute__((ext_vector_type(4)));
typedef __bf16 bf16x2_t __attribute__((ext_vector_type(2)));
typedef unsigned short u16;
typedef unsigned long long u64;
typedef __amdgpu_buffer_rsrc_t rsrc_t;
#define LAS __attribute__((address_space(3)))
#define GAS __attribute__((address_space(1)))
#define RLX_AGENT __ATOMIC_RELAXED, __HIP_MEMORY_SCOPE_AGENT

constexpr int D = 512, F = 1792;
constexpr int NC = 56, NB = 64;
constexpr int AUX_SC1 = 16;
constexpr unsigned SENT = 0xFFFFFFFFu;
constexpr unsigned OOB = 0xC0000000u;
constexpr unsigned SPIN_MAX = 1u << 17;
constexpr float SCALE = 0.08838834764831845f, L2E = 1.4426950408889634f;
constexpr int WS_EPOCH = 0, WS_ROLE = 256, WS_FLAG = 4096  , WS_ML = 0x8000  , WS_PART = 0x10000  ,
              WS_HP = 0x50000  , WS_DP = WS_HP + 32768  , WS_QG = 0xD0000  , WS_BYTES = 0x100000;
constexpr int L_A0 = 0, L_A1 = 8192, L_ML = 16384;
constexpr int L_RED = 20480;
constexpr int L_ST = 28672;
constexpr int L_CS = 29184;
constexpr int L_Q = 31232, L_CTL = 32256;
constexpr int L_HWQ = 32768, L_HXN = 49152;
constexpr int L_W = 32768;
constexpr int L_BYTES = 98304;

__device__ __forceinline__ float u2f(unsigned u) { return __builtin_bit_cast(float, u); }
__device__ __forceinline__ unsigned f2u(float f) { return __builtin_bit_cast(unsigned, f); }
__device__ __forceinline__ unsigned cvtpk(float lo, float hi) { f32x2 v = {lo, hi}; return __builtin_bit_cast(unsigned, __builtin_convertvector(v, bf16x2_t)); }
__device__ __forceinline__ float blo(unsigned u) { return u2f(u << 16); }
__device__ __forceinline__ float bhi(unsigned u) { return u2f(u & 0xffff0000u); }
__device__ __forceinline__ float rbf(float x) { return blo(cvtpk(x, x)); }
template <int CTRL> __device__ __forceinline__ unsigned dppu(unsigned x) { return __builtin_amdgcn_mov_dpp(x, CTRL, 0xf, 0xf, true); }
template <int CTRL> __device__ __forceinline__ float dpp(float x) { return u2f(dppu<CTRL>(f2u(x))); }
constexpr int QX1 = 0xB1, QX2 = 0x4E, ROR4 = 0x124, ROR8 = 0x128, SHL4 = 0x104, SHL8 = 0x108;
__device__ __forceinline__ float xrow_sum(float x) {
  auto s = __builtin_amdgcn_permlane16_swap(f2u(x), f2u(x), false, false);
  x = u2f(s[0]) + u2f(s[1]);
  auto t = __builtin_amdgcn_permlane32_swap(f2u(x), f2u(x), false, false);
  return u2f(t[0]) + u2f(t[1]);
}
__device__ __forceinline__ float xrow_max(float x) {
  auto s = __builtin_amdgcn_permlane16_swap(f2u(x), f2u(x), false, false);
  x = fmaxf(u2f(s[0]), u2f(s[1]));
  auto t = __builtin_amdgcn_permlane32_swap(f2u(x), f2u(x), false, false);
  return fmaxf(u2f(t[0]), u2f(t[1]));
}
__device__ __forceinline__ float sum16_stride4(float x) { x += dpp<ROR4>(x); x += dpp<ROR8>(x); return xrow_sum(x); }
__device__ __forceinline__ float sum64(float x) { x += dpp<QX1>(x); x += dpp<QX2>(x); return sum16_stride4(x); }
__device__ __forceinline__ void tred8(const float (&lo)[4], const float (&hi)[4], float& r0, float& r1) {
  float r4[4];
#pragma unroll
  for (int k = 0; k < 2; ++k) {
    auto s0 = __builtin_amdgcn_permlane32_swap(f2u(lo[k]), f2u(lo[2 + k]), false, false);
    auto s1 = __builtin_amdgcn_permlane32_swap(f2u(hi[k]), f2u(hi[2 + k]), false, false);
    r4[2 * k] = u2f(s0[0]) + u2f(s0[1]); r4[2 * k + 1] = u2f(s1[0]) + u2f(s1[1]);
  }
  auto t0 = __builtin_amdgcn_permlane16_swap(f2u(r4[0]), f2u(r4[2]), false, false);
  auto t1 = __builtin_amdgcn_permlane16_swap(f2u(r4[1]), f2u(r4[3]), false, false);
  r0 = u2f(t0[0]) + u2f(t0[1]); r1 = u2f(t1[0]) + u2f(t1[1]);
}
__device__ __forceinline__ f32x4 mfma32(v4u a, v4u b, f32x4 c) { return __builtin_amdgcn_mfma_f32_16x16x32_bf16(__builtin_bit_cast(s16x8, a), __builtin_bit_cast(s16x8, b), c, 0, 0, 0); }
__device__ __forceinline__ f32x4 mfma16(v2u a, v2u b, f32x4 c) { return __builtin_amdgcn_mfma_f32_16x16x16bf16_1k(__builtin_bit_cast(s16x4, a), __builtin_bit_cast(s16x4, b), c, 0, 0, 0); }
__device__ __forceinline__ float dot2(unsigned a, unsigned b, float acc) { return __builtin_amdgcn_fdot2_f32_bf16(__builtin_bit_cast(bf16x2_t, a), __builtin_bit_cast(bf16x2_t, b), acc, false); }
__device__ __forceinline__ v2u trread(LAS unsigned char* p) { return __builtin_bit_cast(v2u, __builtin_amdgcn_ds_read_tr16_b64_v4i16((LAS s16x4*)p)); }
__device__ __forceinline__ v4u trread2(LAS unsigned char* p, int step) { const v2u a = trread(p), b = trread(p + step); return v4u{a[0], a[1], b[0], b[1]}; }
__device__ __forceinline__ unsigned rfl(unsigned x) { return (unsigned)__builtin_amdgcn_readfirstlane((int)x); }
__device__ __forceinline__ rsrc_t mkrsrc(const void* p, unsigned bytes) { return __builtin_amdgcn_make_buffer_rsrc((void*)p, 0, (int)bytes, 0x00020000); }
__device__ __forceinline__ v4u mksrd(const void* p, unsigned bytes) { const u64 a = (u64)p; return v4u{rfl((unsigned)a), rfl((unsigned)(a >> 32) & 0xffffu), bytes, 0x00020000u}; }
__device__ __forceinline__ v4u ld4(rsrc_t r, unsigned o) { return __builtin_amdgcn_raw_buffer_load_b128(r, (int)o, 0, AUX_SC1); }
__device__ __forceinline__ v2u ld2(rsrc_t r, unsigned o) { return __builtin_amdgcn_raw_buffer_load_b64(r, (int)o, 0, AUX_SC1); }
__device__ __forceinline__ unsigned ld1(rsrc_t r, unsigned o) { return __builtin_amdgcn_raw_buffer_load_b32(r, (int)o, 0, AUX_SC1); }
__device__ __forceinline__ void st4(rsrc_t r, unsigned o, v4u v) { __builtin_amdgcn_raw_buffer_store_b128(v, r, (int)o, 0, AUX_SC1); }
__device__ __forceinline__ void st2(rsrc_t r, unsigned o, v2u v) { __builtin_amdgcn_raw_buffer_store_b64(v, r, (int)o, 0, AUX_SC1); }
__device__ __forceinline__ unsigned san(unsigned u) { return u < 0xFFFFFFFEu ? u : 0xFFFFFFFEu; }
__device__ __forceinline__ v4u pack8(f32x4 a, f32x4 b) { return v4u{san(cvtpk(a[0], a[1])), san(cvtpk(a[2], a[3])), san(cvtpk(b[0], b[1])), san(cvtpk(b[2], b[3]))}; }
__device__ __forceinline__ unsigned umax(unsigned a, unsigned b) { return a > b ? a : b; }
__device__ __forceinline__ unsigned mx4(v4u v) { return umax(umax(v[0], v[1]), umax(v[2], v[3])); }
__device__ __forceinline__ unsigned mx2(v2u v) { return umax(v[0], v[1]); }

#define OPAQUE(x) asm volatile("" : "+v"(x) :: "memory")
#define PIN(x) asm volatile("" : "+v"(x))
#define SB() __builtin_amdgcn_sched_barrier(0)
#define WAITN(N) asm volatile("s_waitcnt vmcnt(%0)" :: "i"(N) : "memory")
#define WAIT4(N, d) asm volatile("s_waitcnt vmcnt(%4)" : "+v"((d)[0]), "+v"((d)[1]), "+v"((d)[2]), "+v"((d)[3]) : "i"(N))
#define BARRIER() do { asm volatile("s_waitcnt lgkmcnt(0)" ::: "memory"); __builtin_amdgcn_s_barrier(); asm volatile("" ::: "memory"); } while (0)
#define LDS_WAIT_GE(p, v, SLEEP) do { _Pragma("unroll 1") for (unsigned spin_ = 0; spin_ < SPIN_MAX * 32u; ++spin_) { \
    if (rfl(*(volatile LAS unsigned*)(p)) >= (v)) break; if (SLEEP) __builtin_amdgcn_s_sleep(1); } asm volatile("" ::: "memory"); } while (0)

__device__ __forceinline__ unsigned lds_ticket(LAS unsigned* p) {
  unsigned t;
  asm volatile("s_mov_b64 exec, 1\n\tds_add_rtn_u32 %0, %1, %2\n\ts_mov_b64 exec, -1\n\ts_waitcnt lgkmcnt(0)" : "=&v"(t) : "v"((unsigned)(size_t)p), "v"(1u) : "memory");
  return rfl(t);
}
__device__ __forceinline__ void aload4(v4u (&d)[4], unsigned voff, v4u srd, unsigned soff) {
  asm volatile("s_nop 7\n\t"
               "buffer_load_dwordx4 %0, %4, %5, %6 offen nt\n\t"
               "buffer_load_dwordx4 %1, %4, %5, %6 offen offset:1024 nt\n\t"
               "buffer_load_dwordx4 %2, %4, %5, %6 offen offset:2048 nt\n\t"
               "buffer_load_dwordx4 %3, %4, %5, %6 offen offset:3072 nt"
               : "=&v"(d[0]), "=&v"(d[1]), "=&v"(d[2]), "=&v"(d[3]) : "v"(voff), "s"(srd), "s"(rfl(soff)));
}
__device__ __forceinline__ void dma1(unsigned ldsoff, unsigned voff, v4u srd, unsigned soff) {
  unsigned keep;
  asm volatile("s_nop 4\n\ts_mov_b32 %0, m0\n\ts_mov_b32 m0, %1\n\ts_nop 0\n\tbuffer_load_dwordx4 %2, %3, %4 offen lds\n\ts_mov_b32 m0, %0"
               : "=&s"(keep) : "s"(rfl(ldsoff)), "v"(voff), "s"(srd), "s"(rfl(soff)) : "memory");
}

__global__ void __launch_bounds__(512, 2) fused_block(const u16* __restrict__ x, const u16* __restrict__ kc, const u16* __restrict__ vc, const u16* __restrict__ wq,
                                                      const u16* __restrict__ wo, const u16* __restrict__ wgate, const u16* __restrict__ wup, const u16* __restrict__ wdn,
                                                      u16* __restrict__ out, unsigned char* ws) {
  __shared__ __attribute__((aligned(16))) unsigned char smem[L_BYTES];
  LAS unsigned char* L = (LAS unsigned char*)smem;
  const int tid = threadIdx.x, lane = tid & 63, w = __builtin_amdgcn_readfirstlane(tid >> 6);
  const int wg = blockIdx.x, xc = wg & 7, y = wg >> 3;
  const int g = lane >> 4, m16 = lane & 15, hq = (lane >> 2) & 3, c = lane & 3;
  LAS unsigned* ctl = (LAS unsigned*)(L + L_CTL);
  const rsrc_t rW = mkrsrc(ws, WS_BYTES);

  if (w < 4) {
    const int cq = xc * 32 + y;
    v4u xv, wv[4]; v2u ep;
    {
      const v4u sX = mksrd(x, 4 * D * 2), sQ = mksrd(wq, D * 4096 * 2), sW = mksrd(ws, 64);
      const unsigned o = (unsigned)(w * 128 + (lane >> 1)) * 8192u + (unsigned)cq * 32u + (lane & 1) * 16;
      const unsigned xo = (unsigned)w * 1024u + lane * 16;
      if (tid < 16) ctl[tid] = 0u;
      asm volatile("s_nop 4\n\tbuffer_load_dwordx4 %0, %6, %7, 0 offen\n\t"
                   "buffer_load_dwordx4 %1, %8, %9, 0 offen\n\t"
                   "buffer_load_dwordx4 %2, %8, %9, %10 offen\n\t"
                   "buffer_load_dwordx4 %3, %8, %9, %11 offen\n\t"
                   "buffer_load_dwordx4 %4, %8, %9, %12 offen\n\t"
                   "buffer_load_dwordx2 %5, off, %13, 0 sc1\n\t"
                   "s_waitcnt lgkmcnt(0)\n\ts_barrier"
                   : "=&v"(xv), "=&v"(wv[0]), "=&v"(wv[1]), "=&v"(wv[2]), "=&v"(wv[3]), "=&v"(ep)
                   : "v"(xo), "s"(sX), "v"(o), "s"(sQ), "s"(32u * 8192u), "s"(64u * 8192u), "s"(96u * 8192u), "s"(sW) : "memory");
    }
    asm volatile("s_waitcnt vmcnt(5)" : "+v"(xv));
    {
      float s1 = 0.f;
#pragma unroll
      for (int k = 0; k < 4; ++k) s1 += blo(xv[k]) + bhi(xv[k]);
      const float mean = sum64(s1) * (1.f / D);
      float s2 = 0.f;
#pragma unroll
      for (int k = 0; k < 4; ++k) { const float a = blo(xv[k]) - mean, b2 = bhi(xv[k]) - mean; s2 = fmaf(a, a, s2); s2 = fmaf(b2, b2, s2); }
      const float var = sum64(s2) * (1.f / D);
      const float mr = rbf(mean), rden = 1.0f / rbf(__builtin_sqrtf(rbf(rbf(var) + 1e-5f)));
      v4u xn;
#pragma unroll
      for (int k = 0; k < 4; ++k) { const unsigned d = cvtpk(blo(xv[k]) - mr, bhi(xv[k]) - mr); xn[k] = cvtpk(blo(d) * rden, bhi(d) * rden); }
      *(LAS v4u*)(L + L_HXN + w * 1024 + lane * 16) = xn;
    }
    asm volatile("s_waitcnt vmcnt(0)" : "+v"(wv[0]), "+v"(wv[1]), "+v"(wv[2]), "+v"(wv[3]), "+v"(ep));
    const u64 tag = (((u64)ep[1] << 32) | ep[0]) + 1ull;
#pragma unroll
    for (int i = 0; i < 4; ++i) *(LAS v4u*)(L + L_HWQ + (w * 4 + i) * 1024 + lane * 16) = wv[i];
    if (tid == 0) *(LAS u64*)(ctl + 6) = tag;
    lds_ticket(ctl + 2);
    LDS_WAIT_GE(ctl + 2, 4u, 0);
    f32x4 qa = {0, 0, 0, 0}, qb = {0, 0, 0, 0};
    {
      v4u a[4];
#pragma unroll
      for (int i = 0; i < 4; ++i) a[i] = *(const LAS v4u*)(L + L_HXN + c * 1024 + (w * 128 + 32 * g) * 2 + i * 16);
      LAS unsigned char* tb = L + L_HWQ + (w * 128 + 32 * g + (m16 >> 2)) * 32 + c * 8;
      qa = mfma32(a[0], trread2(tb, 128), qa);        qb = mfma32(a[1], trread2(tb + 256, 128), qb);
      qa = mfma32(a[2], trread2(tb + 512, 128), qa);  qb = mfma32(a[3], trread2(tb + 768, 128), qb);
      qa += qb;
    }
    *(LAS float*)(L + L_RED + (w * 64 + lane) * 4) = g == 0 ? qa[0] : (g == 1 ? qa[1] : (g == 2 ? qa[2] : qa[3]));
    if (lds_ticket(ctl + 5) == 3) {
      const LAS float* rp = (const LAS float*)(L + L_RED) + lane;
      const float qv = (rp[0] + rp[64]) + (rp[128] + rp[192]);
      __hip_atomic_store((GAS u64*)(ws + WS_QG) + g * 4096 + cq * 16 + m16, (tag << 16) | (u64)(cvtpk(qv, qv) & 0xffffu), RLX_AGENT);
      if (lane == 0) *(volatile LAS unsigned*)(ctl + 3) = 1u;
    }
    LDS_WAIT_GE(ctl + 3, 1u, 1);
    if (w == 0) {
      const v4u sv = {SENT, SENT, SENT, SENT};
      st4(rW, WS_HP + wg * 1024 + lane * 16, sv);
      st4(rW, WS_PART + wg * 1024 + lane * 16, sv);
      st4(rW, lane < 2 ? WS_ML + wg * 128 + lane * 16 : OOB, sv);
      st2(rW, wg == 0 && lane == 0 ? WS_ROLE : OOB, v2u{0u, 0u});
      SB();
    }
  }

  const v4u sK = mksrd(kc, 128u << 20), sV = mksrd(vc, 128u << 20);
  const unsigned kvo = (unsigned)wg * (2048u * 256u) + (unsigned)w * 4096u + (unsigned)(hq * 256 + g * 64 + c * 16);
  const unsigned vvo = (unsigned)wg * (2048u * 256u) + (unsigned)w * 4096u + (unsigned)lane * 16u;
  v4u Kr[2][4], Vr[2][4], P;
  {
    const v4u sW = mksrd(ws, WS_BYTES);
    const unsigned qo = WS_QG + (unsigned)((y >> 3) * 4096 + (y & 7) * 512 + (w & 3) * 128) * 8u + lane * 16;
    unsigned need = w < 4 ? 0u : OOB;
#define PACED() do { if (rfl(*(volatile LAS unsigned*)(ctl + 3)) < 1u) WAITN(4); } while (0)
#define QPOLL() asm volatile("s_nop 4\n\tbuffer_load_dwordx4 %0, %1, %2, %3 offen sc1" : "=&v"(P) : "v"(qo), "s"(sW), "s"(rfl(need)) : "memory")
#define QCHECK() do { const u64 tag = *(const LAS u64*)(ctl + 6); v4u Q = P; PIN(Q); \
      const unsigned thi = (unsigned)(tag >> 16), tlo = ((unsigned)tag & 0xffffu); \
      if (__all(((Q[1] ^ thi) | (Q[3] ^ thi) | ((Q[0] >> 16) ^ tlo) | ((Q[2] >> 16) ^ tlo)) == 0)) { \
        *(LAS unsigned*)(L + L_Q + (w * 64 + lane) * 4) = (Q[0] & 0xffffu) | (Q[2] << 16); lds_ticket(ctl + 4); need = OOB; } } while (0)
    aload4(Kr[0], kvo, sK, 0u);
    if (w >= 4) __builtin_amdgcn_s_barrier();
    aload4(Vr[0], vvo, sV, 0u);
    QPOLL();
    PACED();
    aload4(Kr[1], kvo, sK, 32768u);
    PACED();
    aload4(Vr[1], vvo, sV, 32768u);
    if (need == 0u) { WAITN(8); SB(); QCHECK(); }
#pragma unroll 1
    for (unsigned spin = 0; spin < SPIN_MAX && need == 0u; ++spin) { QPOLL(); WAITN(0); SB(); QCHECK(); }
  }
  LDS_WAIT_GE(ctl + 4, 4u, 1);
  const v4u qf = *(const LAS v4u*)(L + L_Q + (hq * 128 + g * 32 + c * 8) * 2);
  const unsigned mask0 = c == 0 ? 0x0000ffffu : (c == 1 ? 0xffff0000u : 0u);
  const unsigned mask1 = c == 2 ? 0x0000ffffu : (c == 3 ? 0xffff0000u : 0u);
  float mrun = -INFINITY, lrun = 0.f;
  f32x4 acc0 = {0, 0, 0, 0}, acc1 = {0, 0, 0, 0};
#define PROCESS(s, NK, NV, ISSUE, so)                                                                                   \
  {                                                                                                                     \
    WAIT4(NK, Kr[s]);                                                                                                   \
    float sc[4];                                                                                                        \
    _Pragma("unroll") for (int t = 0; t < 4; ++t) {                                                                     \
      const f32x4 d = mfma32(Kr[s][t], qf, f32x4{0, 0, 0, 0});              \
      float v = dpp<0x00>(d[0]) + dpp<0x55>(d[1]);                                            \
      v += dpp<0xAA>(d[2]);                                                                                             \
      v += dpp<0xFF>(d[3]);                                                                                             \
      sc[t] = v;                                                                                                        \
    }                                                                                                                   \
    SB();                                                                                                               \
    if (ISSUE) aload4(Kr[s], kvo, sK, so);                                                                              \
    SB();                                                                                                               \
    _Pragma("unroll") for (int t = 0; t < 4; t += 2) {                                                                  \
      const unsigned p1 = cvtpk(sc[t], sc[t + 1]);                                                                      \
      const unsigned p2 = cvtpk(blo(p1) * SCALE, bhi(p1) * SCALE);                                                      \
      sc[t] = blo(p2); sc[t + 1] = bhi(p2);                                                                             \
    }                                                                                                                   \
    const float mn = fmaxf(mrun, xrow_max(fmaxf(fmaxf(sc[0], sc[1]), fmaxf(sc[2], sc[3]))));                            \
    const float alpha = __builtin_amdgcn_exp2f((mrun - mn) * L2E);                                                      \
    mrun = mn;                                                                                                          \
    lrun *= alpha; acc0 *= alpha; acc1 *= alpha;                                                                        \
    const float mb = -mn * L2E;                                                                                         \
    v2u pbs[4];                                                                                                         \
    _Pragma("unroll") for (int t = 0; t < 4; ++t) {                                                                     \
      const float p = __builtin_amdgcn_exp2f(fmaf(sc[t], L2E, mb));                                                     \
      lrun += p;                                                                                                        \
      const unsigned pk = cvtpk(p, p);                                                                                  \
      pbs[t] = v2u{pk & mask0, pk & mask1};                                                                             \
    }                                                                                                                   \
    SB();                                                                                                               \
    WAIT4(NV, Vr[s]);                                                                                                   \
    _Pragma("unroll") for (int t = 0; t < 4; ++t) {                                                                     \
      acc0 = mfma16(v2u{Vr[s][t][0], Vr[s][t][1]}, pbs[t], acc0);                                                       \
      acc1 = mfma16(v2u{Vr[s][t][2], Vr[s][t][3]}, pbs[t], acc1);                                                       \
    }                                                                                                                   \
    SB();                                                                                                               \
    if (ISSUE) aload4(Vr[s], vvo, sV, so);                                                                              \
    SB();                                                                                                               \
  }
#pragma unroll 1
  for (int j = 2; j < 16; j += 2) {
    PROCESS(0, 12, 12, true, (unsigned)j * 32768u)
    PROCESS(1, 12, 12, true, (unsigned)(j + 1) * 32768u)
    if (j == 2 && tid == 0) __hip_atomic_store((GAS u64*)(ws + WS_FLAG) + wg, *(const LAS u64*)(ctl + 6), RLX_AGENT);
  }
  unsigned lrec = w * 1024 + lane * 16, lml = L_ML + w * 512 + lane * 8;
  unsigned ppo = WS_PART + wg * 1024 + hq * 256 + g * 64 + c * 16, pmo = (g == 0 && c == 0 ? 0u : OOB) + WS_ML + wg * 128 + hq * 8;
  PIN(lrec); PIN(lml); PIN(ppo); PIN(pmo);
  PROCESS(0, 12, 8, false, 0u)
  PROCESS(1, 4, 0, false, 0u)
  asm volatile("" :: "v"(P));

  *(LAS f32x4*)(L + L_A0 + lrec) = acc0;
  *(LAS f32x4*)(L + L_A1 + lrec) = acc1;
  *(LAS f32x2*)(L + lml) = f32x2{mrun, lrun};
  const int vw = lds_ticket(ctl);
  if (vw == 7) {
    f32x2 ml[8]; f32x4 r0[8], r1[8]; float M = -INFINITY;
#pragma unroll
    for (int ww = 0; ww < 8; ++ww) ml[ww] = *(const LAS f32x2*)(L + L_ML + ww * 512 + lane * 8);
#pragma unroll
    for (int ww = 0; ww < 8; ++ww) { r0[ww] = *(const LAS f32x4*)(L + L_A0 + ww * 1024 + lane * 16); r1[ww] = *(const LAS f32x4*)(L + L_A1 + ww * 1024 + lane * 16); }
    SB();
#pragma unroll
    for (int ww = 0; ww < 8; ++ww) M = fmaxf(M, ml[ww][0]);
    float wt[8], Ls = 0.f;
#pragma unroll
    for (int ww = 0; ww < 8; ++ww) { wt[ww] = __builtin_amdgcn_exp2f((ml[ww][0] - M) * L2E); Ls = fmaf(wt[ww], ml[ww][1], Ls); }
    f32x4 o0 = wt[0] * r0[0], o1 = wt[0] * r1[0];
#pragma unroll
    for (int ww = 1; ww < 8; ++ww) { o0 += wt[ww] * r0[ww]; o1 += wt[ww] * r1[ww]; }
    Ls = xrow_sum(Ls);
    const float inv = __builtin_amdgcn_rcpf(Ls);
    o0 *= inv; o1 *= inv;
    st4(rW, ppo, pack8(o0, o1));
    st2(rW, pmo, v2u{san(f2u(M)), san(f2u(Ls))});
  }
  if (vw == 0) {
    const u64 tag = *(const LAS u64*)(ctl + 6);
    const unsigned tl = (unsigned)tag, th = (unsigned)(tag >> 32);
    unsigned fo = WS_FLAG + lane * 32;
#pragma unroll 1
    for (unsigned spin = 0; spin < SPIN_MAX; ++spin) {
      OPAQUE(fo);
      const v4u a = ld4(rW, fo), b = ld4(rW, fo + 16);
      if (__all(((a[0] ^ tl) | (a[1] ^ th) | (a[2] ^ tl) | (a[3] ^ th) | (b[0] ^ tl) | (b[1] ^ th) | (b[2] ^ tl) | (b[3] ^ th)) == 0)) break;
      __builtin_amdgcn_s_sleep(8);
    }
    LDS_WAIT_GE(ctl, 7u, 1);
    if (lane == 0) {
      const unsigned r = __hip_atomic_fetch_add((GAS unsigned*)(ws + WS_ROLE), 1u, RLX_AGENT);
      if (r == 0) __hip_atomic_store((GAS u64*)(ws + WS_EPOCH), tag, RLX_AGENT);
      *(volatile LAS unsigned*)(ctl + 1) = r + 1u;
    }
  }
  LDS_WAIT_GE(ctl + 1, 1u, 1);
  const unsigned role = rfl(*(volatile LAS unsigned*)(ctl + 1)) - 1u;
  if (role >= NC + NB) return;
  const unsigned ones = 0x3F803F80u;

  if (role >= NC) {
    const int bj = role - NC, kp = bj >> 3, cg = bj & 7;
    if (vw < 4) {
      const v4u sO = mksrd(wo, 4096 * D * 2);
      const int p = lane >> 3;
      const unsigned o = (unsigned)((kp * 4 + vw) * 128 + 8 * (p & 3) + (p & 4)) * 1024u + cg * 128 + (lane & 7) * 16;
#pragma unroll 1
      for (int k = 0; k < 16; ++k) dma1(L_W + (vw * 16 + k) * 1024, o, sO, (unsigned)(32 * (k >> 2) + (k & 3)) * 1024u);
      WAITN(0);
      lds_ticket(ctl + 9);
    }
    LDS_WAIT_GE(ctl + 9, 4u, 1);
    v4u fr[4][2];
    {
      LAS unsigned char* tb = L + L_W + (64 * vw + 8 * g + (m16 >> 2)) * 128 + c * 8;
#pragma unroll
      for (int n = 0; n < 4; ++n)
#pragma unroll
        for (int s2 = 0; s2 < 2; ++s2) { fr[n][s2] = trread2(tb + n * 32 + s2 * 4096, 512); PIN(fr[n][s2]); }
    }
    unsigned lw = L_RED + vw * 1024 + (g * 4 + m16) * 16, hpo = WS_HP + (cg * 8 + kp) * 512 + lane * 8, lr = L_RED + lane * 16;
    PIN(lw); PIN(hpo); PIN(lr);
    const int sph = (lane >> 3) & 1, st = (lane >> 2) & 1;
    unsigned po = WS_PART + (unsigned)(((c * 8 + kp) * 8 + 4 * sph) * 1024 + vw * 128 + (4 * st + g) * 16);
    unsigned mo = WS_ML + (unsigned)(((c * 8 + kp) * 8 + 4 * sph + g) * 128 + (vw >> 1) * 8);
    v4u a[4]; v2u ml = {SENT, SENT};
#pragma unroll
    for (int i = 0; i < 4; ++i) a[i] = v4u{SENT, SENT, SENT, SENT};
#pragma unroll 1
    for (unsigned spin = 0; spin < SPIN_MAX; ++spin) {
      OPAQUE(mo);
      ml = ld2(rW, mo);
      if (__builtin_popcountll(__ballot(ml[0] == SENT)) <= 16) break;
      __builtin_amdgcn_s_sleep(4);
    }
#pragma unroll 1
    for (unsigned spin = 0; spin < SPIN_MAX; ++spin) {
      OPAQUE(po); OPAQUE(mo);
#pragma unroll
      for (int i = 0; i < 4; ++i) if (mx4(a[i]) == SENT) a[i] = ld4(rW, po + i * 1024);
      if (mx2(ml) == SENT) ml = ld2(rW, mo);
      if (__all(umax(umax(umax(mx4(a[0]), mx4(a[1])), umax(mx4(a[2]), mx4(a[3]))), mx2(ml)) != SENT)) break;
    }
    float M = u2f(ml[0]);
    M = fmaxf(M, dpp<ROR8>(M)); M = xrow_max(M);
    float wa = __builtin_amdgcn_exp2f((u2f(ml[0]) - M) * L2E) * u2f(ml[1]);
    float Ls = wa + dpp<ROR8>(wa); Ls = xrow_sum(Ls);
    wa *= __builtin_amdgcn_rcpf(Ls);
    float wr[4];
    {
      auto s1 = __builtin_amdgcn_permlane16_swap(f2u(wa), f2u(wa), false, false);
      auto t0 = __builtin_amdgcn_permlane32_swap(s1[0], s1[0], false, false);
      auto t1 = __builtin_amdgcn_permlane32_swap(s1[1], s1[1], false, false);
      wr[0] = u2f(t0[0]); wr[2] = u2f(t0[1]); wr[1] = u2f(t1[0]); wr[3] = u2f(t1[1]);
    }
    v4u oa;
#pragma unroll
    for (int k = 0; k < 4; ++k) {
      float lo = (wr[0] * blo(a[0][k]) + wr[1] * blo(a[1][k])) + (wr[2] * blo(a[2][k]) + wr[3] * blo(a[3][k]));
      float hi = (wr[0] * bhi(a[0][k]) + wr[1] * bhi(a[1][k])) + (wr[2] * bhi(a[2][k]) + wr[3] * bhi(a[3][k]));
      lo += dpp<ROR8>(lo); hi += dpp<ROR8>(hi);
      oa[k] = cvtpk(lo, hi);
    }
    const v4u ob = {dppu<SHL4>(oa[0]), dppu<SHL4>(oa[1]), dppu<SHL4>(oa[2]), dppu<SHL4>(oa[3])};
    f32x4 dd[4];
#pragma unroll
    for (int n = 0; n < 4; ++n) dd[n] = mfma32(fr[n][0], oa, f32x4{0, 0, 0, 0});
#pragma unroll
    for (int n = 0; n < 4; ++n) dd[n] = mfma32(fr[n][1], ob, dd[n]);
    if (m16 < 4) {
#pragma unroll
      for (int n = 0; n < 4; ++n) *(LAS f32x4*)(L + lw + n * 256) = dd[n];
    }
    const int tk = lds_ticket(ctl + 8);
    if (tk == 7) {
      const LAS f32x4* rp = (const LAS f32x4*)(L + lr);
      const f32x4 t = ((rp[0] + rp[64]) + (rp[128] + rp[192])) + ((rp[256] + rp[320]) + (rp[384] + rp[448]));
      st2(rW, hpo, v2u{san(cvtpk(t[0], t[1])), san(cvtpk(t[2], t[3]))});
    } else if (tk < 2) {
      const int pc = 2 * bj + tk, b = pc >> 5, pg = lane >> 1, c2 = lane & 1;
      const unsigned xr = *(const GAS unsigned*)(x + pc * 16 + c2 * 8 + g * 2);
      float hlo, hhi;
      {
        const int col = (pc & 31) * 16 + 8 * c2 + 2 * g;
        unsigned ho = WS_HP + (unsigned)(((col >> 6) * 8 + (pg & 7)) * 512 + ((col & 63) >> 2) * 32 + b * 8 + (col & 3) * 2);
        unsigned hv = 0;
#pragma unroll 1
        for (unsigned spin = 0; spin < SPIN_MAX; ++spin) {
          __builtin_amdgcn_s_sleep(2);
          OPAQUE(ho);
          hv = ld1(rW, ho);
          if (__all(hv != SENT)) break;
        }
        float r0 = blo(hv), r1 = bhi(hv);
        r0 += dpp<QX2>(r0); r1 += dpp<QX2>(r1); r0 += dpp<ROR4>(r0); r1 += dpp<ROR4>(r1); r0 += dpp<ROR8>(r0); r1 += dpp<ROR8>(r1);
        const unsigned at = cvtpk(r0, r1);
        const unsigned hp = cvtpk(blo(xr) + blo(at), bhi(xr) + bhi(at));
        hlo = blo(hp); hhi = bhi(hp);
      }
      unsigned ro = WS_DP + (unsigned)(pg * 4096 + pc * 32 + c2 * 16);
      unsigned ro1 = pg < NC - 32 ? ro + 32 * 4096 : OOB;
      const rsrc_t rOut = mkrsrc(out, 4 * D * 2);
      unsigned oo = (m16 < 2 ? 0u : OOB) + (pc * 16 + c2 * 8 + g * 2) * 2;
      PIN(oo); PIN(hlo); PIN(hhi);
      __builtin_amdgcn_s_sleep(60);
      v4u p0 = {SENT, SENT, SENT, SENT}, p1 = p0;
#pragma unroll 1
      for (unsigned spin = 0; spin < SPIN_MAX; ++spin) {
        const bool miss = umax(mx4(p0), mx4(p1)) == SENT;
        if (__ballot(miss) == 0) break;
        OPAQUE(ro); OPAQUE(ro1);
        if (miss) { p0 = ld4(rW, ro); p1 = ld4(rW, ro1); }
      }
      float r0, r1;
      { const float lo[4] = {blo(p0[0]) + blo(p1[0]), blo(p0[1]) + blo(p1[1]), blo(p0[2]) + blo(p1[2]), blo(p0[3]) + blo(p1[3])},
                    hi[4] = {bhi(p0[0]) + bhi(p1[0]), bhi(p0[1]) + bhi(p1[1]), bhi(p0[2]) + bhi(p1[2]), bhi(p0[3]) + bhi(p1[3])}; tred8(lo, hi, r0, r1); }
      r0 += dpp<ROR8>(r0); r1 += dpp<ROR8>(r1); r0 += dpp<ROR4>(r0); r1 += dpp<ROR4>(r1); r0 += dpp<QX2>(r0); r1 += dpp<QX2>(r1);
      const unsigned dn = cvtpk(r0, r1);
      __builtin_amdgcn_raw_buffer_store_b32(cvtpk(hlo + blo(dn), hhi + bhi(dn)), rOut, (int)oo, 0, 0);
    }
  } else {
    const int ci = role;
    v4u wp[8];
    if (vw < 4) {
      const v4u sG = mksrd(wgate, D * F * 2), sU = mksrd(wup, D * F * 2);
      const unsigned o = (unsigned)(128 * vw + (lane >> 2)) * (F * 2) + ci * 64 + (lane & 3) * 16;
#pragma unroll 1
      for (int i = 0; i < 8; ++i) { dma1(L_W + (vw * 8 + i) * 1024, o, sG, i * 16u * F * 2); dma1(L_W + 32768 + (vw * 8 + i) * 1024, o, sU, i * 16u * F * 2); }
      const rsrc_t rD = mkrsrc(wdn, F * D * 2);
      const unsigned od = (unsigned)(ci * 32 + 4 * g) * 1024u + vw * 256 + m16 * 16;
      v4u wt[8];
#pragma unroll
      for (int k = 0; k < 8; ++k) wt[k] = __builtin_amdgcn_raw_buffer_load_b128(rD, (int)(od + ((k >> 2) * 16 + (k & 3)) * 1024), 0, 0);
#pragma unroll
      for (int k = 0; k < 8; k += 2) { wp[k] = v4u{wt[k][0], wt[k][1], wt[k + 1][0], wt[k + 1][1]}; wp[k + 1] = v4u{wt[k][2], wt[k][3], wt[k + 1][2], wt[k + 1][3]}; PIN(wp[k]); PIN(wp[k + 1]); }
      WAITN(0);
      lds_ticket(ctl + 9);
    }
    LDS_WAIT_GE(ctl + 9, 4u, 1);
    v4u fr[4][2];
    {
      LAS unsigned char* tb = L + L_W + (64 * vw + 16 * g + (m16 >> 2)) * 64 + c * 8;
      f32x4 cs[4];
#pragma unroll
      for (int n = 0; n < 4; ++n) {
        cs[n] = f32x4{0, 0, 0, 0};
#pragma unroll
        for (int s2 = 0; s2 < 2; ++s2) {
          fr[n][s2] = trread2(tb + (n & 1) * 32768 + (n >> 1) * 32 + s2 * 512, 256);
          cs[n] = mfma32(v4u{ones, ones, ones, ones}, fr[n][s2], cs[n]);
        }
      }
      *(LAS float*)(L + L_CS + (vw * 64 + lane) * 4) = g == 0 ? cs[0][0] : (g == 1 ? cs[1][0] : (g == 2 ? cs[2][0] : cs[3][0]));
    }
    BARRIER();
    float cs4[4] = {0.f, 0.f, 0.f, 0.f};
    {
      const LAS float* cp = (const LAS float*)(L + L_CS) + 4 * g + c;
#pragma unroll
      for (int ww = 0; ww < 8; ++ww)
#pragma unroll
        for (int n = 0; n < 4; ++n) cs4[n] += cp[ww * 64 + n * 16];
      PIN(cs4[0]); PIN(cs4[1]); PIN(cs4[2]); PIN(cs4[3]);
    }
    unsigned lw = L_RED + (vw * 64 + m16) * 16, ls = L_ST + (c * 8 + vw) * 8, lsr = L_ST + hq * 64, lrr = L_RED + ((4 * g + c) * 4 + hq) * 4;
    unsigned dpo = WS_DP + (unsigned)(ci * 4096 + g * 1024 + vw * 256 + m16 * 16);
    PIN(lw); PIN(ls); PIN(lsr); PIN(lrr); PIN(dpo);
    const v2u xr = *(const GAS v2u*)(x + c * D + 64 * vw + 16 * g + 4 * hq);
    float xf[4] = {blo(xr[0]), bhi(xr[0]), blo(xr[1]), bhi(xr[1])};
    PIN(xf[0]); PIN(xf[1]); PIN(xf[2]); PIN(xf[3]);
    unsigned ho = WS_HP + (unsigned)(vw * 4096 + lane * 8);
    {
      unsigned to = lane < 8 ? WS_HP + (unsigned)(vw * 4096 + lane * 512) : OOB;
#pragma unroll 1
      for (unsigned spin = 0; spin < SPIN_MAX; ++spin) {
        OPAQUE(to);
        if (__builtin_popcountll(__ballot(ld1(rW, to) == SENT)) <= 3) break;
        __builtin_amdgcn_s_sleep(1);
      }
    }
    v2u p[8];
#pragma unroll
    for (int i = 0; i < 8; ++i) p[i] = v2u{SENT, SENT};
#pragma unroll 1
    for (unsigned spin = 0; spin < SPIN_MAX; ++spin) {
      OPAQUE(ho);
      unsigned mx = 0;
#pragma unroll
      for (int i = 0; i < 8; ++i) if (mx2(p[i]) == SENT) p[i] = ld2(rW, ho + i * 512);
#pragma unroll
      for (int i = 0; i < 8; ++i) mx = umax(mx, mx2(p[i]));
      if (__all(mx != SENT)) break;
    }
    unsigned hh[2];
#pragma unroll
    for (int k = 0; k < 2; ++k) {
      const float lo = ((blo(p[0][k]) + blo(p[1][k])) + (blo(p[2][k]) + blo(p[3][k]))) + ((blo(p[4][k]) + blo(p[5][k])) + (blo(p[6][k]) + blo(p[7][k])));
      const float hi = ((bhi(p[0][k]) + bhi(p[1][k])) + (bhi(p[2][k]) + bhi(p[3][k]))) + ((bhi(p[4][k]) + bhi(p[5][k])) + (bhi(p[6][k]) + bhi(p[7][k])));
      const unsigned at = cvtpk(lo, hi);
      hh[k] = cvtpk(xf[2 * k] + blo(at), xf[2 * k + 1] + bhi(at));
    }
    const v4u a0 = {hh[0], hh[1], dppu<SHL4>(hh[0]), dppu<SHL4>(hh[1])};
    const v4u a1 = {dppu<SHL8>(a0[0]), dppu<SHL8>(a0[1]), dppu<SHL8>(a0[2]), dppu<SHL8>(a0[3])};
    f32x4 dd[4];
#pragma unroll
    for (int n = 0; n < 4; ++n) dd[n] = mfma32(a0, fr[n][0], f32x4{0, 0, 0, 0});
#pragma unroll
    for (int n = 0; n < 4; ++n) dd[n] = mfma32(a1, fr[n][1], dd[n]);
    const float s1 = sum16_stride4(dot2(hh[0], ones, dot2(hh[1], ones, 0.f)));
    const float s2 = sum16_stride4(dot2(hh[0], hh[0], dot2(hh[1], hh[1], 0.f)));
    if (lane < 16) {
#pragma unroll
      for (int n = 0; n < 4; ++n) *(LAS f32x4*)(L + lw + n * 256) = dd[n];
    }
    if (lane < 4) *(LAS v2u*)(L + ls) = v2u{f2u(s1), f2u(s2)};
    BARRIER();
    if (vw < 4) {
      const LAS f32x4* sp = (const LAS f32x4*)(L + lsr);
      const f32x4 sq0 = sp[0], sq1 = sp[1], sq2 = sp[2], sq3 = sp[3];
      const LAS float* rp = (const LAS float*)(L + lrr);
      f32x2 pa[2][8];
#pragma unroll
      for (int ww = 0; ww < 8; ++ww)
#pragma unroll
        for (int n = 0; n < 2; ++n) pa[n][ww] = f32x2{rp[(ww * 4 + 2 * n) * 64], rp[(ww * 4 + 2 * n + 1) * 64]};
      SB();
      const f32x4 qs = (sq0 + sq1) + (sq2 + sq3);
      f32x2 sm[2];
#pragma unroll
      for (int n = 0; n < 2; ++n) sm[n] = ((pa[n][0] + pa[n][1]) + (pa[n][2] + pa[n][3])) + ((pa[n][4] + pa[n][5]) + (pa[n][6] + pa[n][7]));
      const float mean = (qs[0] + qs[2]) * (1.f / D);
      const float var = fmaxf((qs[1] + qs[3]) * (1.f / D) - mean * mean, 0.f);
      const float mr = rbf(mean);
      const float rden = __builtin_amdgcn_rcpf(rbf(__builtin_amdgcn_sqrtf(rbf(rbf(var) + 1e-5f))));
      f32x4 o0 = {0, 0, 0, 0}, o1 = {0, 0, 0, 0};
#pragma unroll
      for (int n = 0; n < 2; ++n) {
        const unsigned gu = cvtpk((sm[n][0] - mr * cs4[2 * n]) * rden, (sm[n][1] - mr * cs4[2 * n + 1]) * rden);
        const float gs = blo(gu), us = bhi(gu);
        const float e = __builtin_amdgcn_exp2f(gs * fmaf(gs * gs, 0.044715f * 0.7978845608028654f * 2.f * L2E, 0.7978845608028654f * 2.f * L2E));
        const float act = rbf(gs - gs * __builtin_amdgcn_rcpf(e + 1.f)) * us;
        const unsigned pk = cvtpk(act, act);
        const unsigned p0 = dppu<0x00>(pk), p1 = dppu<0x55>(pk), p2 = dppu<0xAA>(pk), p3 = dppu<0xFF>(pk);
        const v4u ua = {p0 & mask0, p0 & mask1, p1 & mask0, p1 & mask1}, ub = {p2 & mask0, p2 & mask1, p3 & mask0, p3 & mask1};
        o0 = mfma32(ua, wp[n * 4], o0);     o1 = mfma32(ua, wp[n * 4 + 1], o1);
        o0 = mfma32(ub, wp[n * 4 + 2], o0); o1 = mfma32(ub, wp[n * 4 + 3], o1);
      }
      st4(rW, dpo, pack8(o0, o1));
    }
  }
}

extern "C" void kernel_launch(void* const* d_in, const int* in_sizes, int n_in, void* d_out, int out_size, void* d_ws, size_t ws_size, hipStream_t stream) {
  fused_block<<<256, 512, 0, stream>>>((const u16*)d_in[0], (const u16*)d_in[1], (const u16*)d_in[2], (const u16*)d_in[3], (const u16*)d_in[4],
                                       (const u16*)d_in[6], (const u16*)d_in[7], (const u16*)d_in[8], (u16*)d_out, (unsigned char*)d_ws);
}
```
